```python
import torch

def _softmax(x, axis=-1):
    return torch.softmax(x, dim=axis)

def _gelu(x):
    return torch.nn.functional.gelu(x, approximate='tanh')

def _silu(x):
    return torch.nn.functional.silu(x)

def _sigmoid(x):
    return torch.sigmoid(x)

def _layernorm(x, axis=-1, eps=1e-05):
    m = x.mean(dim=axis, keepdim=True)
    v = x.var(dim=axis, keepdim=True, unbiased=False)
    return (x - m) / torch.sqrt(v + eps)

def _scatter_add(tgt, idx, src, axis=0):
    return tgt.clone().index_add_(axis, idx, src)

def _segment_sum(src, seg, n):
    out = torch.zeros((n,) + src.shape[1:], dtype=src.dtype)
    return out.index_add_(0, seg, src)

def _pad(x, pw):
    flat = tuple((v for pair in reversed(pw) for v in pair))
    return torch.nn.functional.pad(x, flat)
kernel_name = 'adaLN_cond__gqa_block_cached__glu_gated_geglu__decode'

def setup_inputs():

    _w = lambda a: (lambda t: t.to(torch.bfloat16) if t.is_floating_point() else t)(torch.as_tensor(a))

    def _A_setup():
        B, Sq, D = (1, 1, 1024)
        Hq, Hkv, Sk, Dh = (32, 4, 65536, 128)
        assert Hq % Hkv == 0, (Hq, Hkv)
        return {'x': _w((lambda *s: torch.randn(*s, dtype=torch.bfloat16) * 0.05)(B, Sq, D)), 'k_cache': _w((lambda *s: torch.randn(*s, dtype=torch.bfloat16) * 3.2)(B, Hkv, Sk, Dh)), 'v_cache': _w((lambda *s: torch.randn(*s, dtype=torch.bfloat16) * 3.2)(B, Hkv, Sk, Dh)), 'wq': _w((lambda *s: torch.randn(*s, dtype=torch.bfloat16) * 3.2)(D, Hq * Dh) * D ** (-0.5)), 'wo': _w((lambda *s: torch.randn(*s, dtype=torch.bfloat16) * 3.2)(Hq * Dh, D) * (Hq * Dh) ** (-0.5))}
    _A_in = _A_setup()

    def _B_setup():
        B, S, Dm, Df = (1, 1, 1024, 3584)
        return {'x': _w((lambda *s: torch.randn(*s, dtype=torch.bfloat16) * 0.05)(B, S, Dm)), 'w_gate': _w((lambda *s: torch.randn(*s, dtype=torch.bfloat16) * 3.2)(Dm, Df) * Dm ** (-0.5)), 'w_up': _w((lambda *s: torch.randn(*s, dtype=torch.bfloat16) * 3.2)(Dm, Df) * Dm ** (-0.5)), 'w_down': _w((lambda *s: torch.randn(*s, dtype=torch.bfloat16) * 3.2)(Df, Dm) * Df ** (-0.5))}
    _B_in = _B_setup()
    return {'A_x': _A_in['x'], 'A_k_cache': _A_in['k_cache'], 'A_v_cache': _A_in['v_cache'], 'A_wq': _A_in['wq'], 'A_wo': _A_in['wo'], 'B_x': _B_in['x'], 'B_w_gate': _B_in['w_gate'], 'B_w_up': _B_in['w_up'], 'B_w_down': _B_in['w_down'], 'c': _w((lambda *s: torch.randn(*s, dtype=torch.bfloat16) * 0.4)(1, 1024) * 8.0), 'w_mod': _w((lambda *s: torch.randn(*s, dtype=torch.bfloat16) * 0.4)(1024, 6 * 1024) * 1024 ** (-0.5) * 8.0)}

def reference(A_x, A_k_cache, A_v_cache, A_wq, A_wo, B_x, B_w_gate, B_w_up, B_w_down, c, w_mod):

    def _A_ref(x, k_cache, v_cache, wq, wo):
        B, Sq, D = x.shape
        Hq, Dh = (32, 128)
        Hkv = k_cache.shape[1]
        rep = Hq // Hkv
        q = (lambda x, *d: x.permute(*d))((x @ wq).reshape(B, Sq, Hq, Dh), 0, 2, 1, 3)
        k = (lambda x, n, axis: torch.repeat_interleave(x, n, dim=axis))(k_cache, rep, axis=1)
        v = (lambda x, n, axis: torch.repeat_interleave(x, n, dim=axis))(v_cache, rep, axis=1)
        scores = torch.einsum('bhqd,bhkd->bhqk', q, k) * Dh ** (-0.5)
        p = _softmax(scores, axis=-1)
        o = torch.einsum('bhqk,bhkd->bhqd', p, v)
        return (lambda x, *d: x.permute(*d))(o, 0, 2, 1, 3).reshape(B, Sq, Hq * Dh) @ wo

    def _B_ref(x, w_gate, w_up, w_down):
        g = torch.einsum('bsd,df->bsf', x, w_gate)
        u = torch.einsum('bsd,df->bsf', x, w_up)
        h = _gelu(g) * u
        return torch.einsum('bsf,fd->bsd', h, w_down)
    x = A_x
    mod = (_silu(c) @ w_mod).reshape(c.shape[0], 6, 1, -1)
    sa, ca, ga, sb, cb, gb = (mod[:, 0].clone(), mod[:, 1].clone(), mod[:, 2].clone(), mod[:, 3].clone(), mod[:, 4].clone(), mod[:, 5].clone())
    h = _layernorm(x, axis=-1) * (1 + ca) + sa
    x = x + ga * _A_ref(h, A_k_cache, A_v_cache, A_wq, A_wo)
    h = _layernorm(x, axis=-1) * (1 + cb) + sb
    x = x + gb * _B_ref(h, B_w_gate, B_w_up, B_w_down)
    return x
```

```cpp
#include <hip/hip_runtime.h>
#include <stdint.h>

#define LAS __attribute__((address_space(3)))
typedef unsigned v4u __attribute__((ext_vector_type(4)));
typedef unsigned v8u __attribute__((ext_vector_type(8)));
typedef __bf16 bf16x8 __attribute__((ext_vector_type(8)));
typedef __bf16 bf16x16 __attribute__((ext_vector_type(16)));
typedef unsigned v2u __attribute__((ext_vector_type(2)));
typedef float f32x4 __attribute__((ext_vector_type(4)));
typedef short s16x8 __attribute__((ext_vector_type(8)));
typedef short s16x4 __attribute__((ext_vector_type(4)));
typedef __bf16 bf16x2 __attribute__((ext_vector_type(2)));
typedef float f32x2 __attribute__((ext_vector_type(2)));
typedef unsigned short u16;
typedef __amdgpu_buffer_rsrc_t rsrc_t;

constexpr int NBLK = 32;
constexpr int WIN = 23;
constexpr int WIN0 = 60, WINQ = 22, WINP = 40;
constexpr int NWP = 9, NWQ = 7;
constexpr int NSEAM = 2, NPRE = 3;
constexpr int ESLEEP = 14, RSLEEP = 32, QSLEEP1 = 30, QSLEEP2 = 112, ABSLEEP = 10, FSLEEP = 30;

constexpr unsigned SENT = 0xFFFFFFFFu;
constexpr int OOB = 0x40000000;
constexpr int SC1 = 16;
constexpr float L2E = 1.4426950408889634f;
constexpr int T_PA = 0, T_PW = T_PA + 524288, T_P1 = T_PW + 32768, T_P2 = T_P1 + 131072, T_P3 = T_P2 + 524288, T_PROBE = T_P3 + 131072,
              WS_M1 = T_PROBE + 65536, WS_Q1 = WS_M1 + 131072, WS_M2 = WS_Q1 + 262144, WS_QF = WS_M2 + 131072, WS_EPOCH = WS_QF + 32768,
              WS_PI = WS_EPOCH + 2048, T_AB = 2 << 20, T_END = 3 << 20;
static_assert(WS_PI + 128 <= T_AB, "everything but the last area inside one 2 MiB page");
constexpr int HS = 8192, NHS = 19, SCR = NHS * HS, L_FULL = SCR, L_CONS = SCR + 16, L_CLR = SCR + 32, L_SYNC = SCR + 36, L_TAG = SCR + 40,
              L_QRDY = SCR + 44, L_MRDY = SCR + 48, L_ZERO = SCR + 64, L_ST = SCR + 384, L_X1 = SCR + 448, L_Y = SCR + 576,
              L_MOD = SCR + 1024, L_PRIV = SCR + 2048, L_Q = SCR + 3072, L_REC = SCR + 5120, L_H = L_REC, LDS_BYTES = SCR + 8192;
constexpr int DN0 = (10 + NWP + 2 * NBLK) % NHS, L_ABP = (DN0 + 7) * HS, L_RED = (DN0 + 8) * HS;
constexpr int UP0 = (10 + NWQ + 2 * NBLK) % NHS;
static_assert(DN0 + 9 <= NHS && UP0 + 7 <= NHS, "the tiles behind the stream do not wrap round the ring");
constexpr int HEAD = 10;
constexpr unsigned LIM_LDS = 1u << 21, LIM_GLB = 1u << 18;

struct Params {
  const u16 *x, *K, *V, *wq, *wo, *wg, *wu, *wd, *c, *wmod;
  u16* out;
  unsigned char* ws;
};

__device__ __forceinline__ float asf(unsigned u) { return __builtin_bit_cast(float, u); }
__device__ __forceinline__ unsigned asu(float f) { return __builtin_bit_cast(unsigned, f); }
__device__ __forceinline__ float lo16(unsigned u) { return __builtin_bit_cast(float, u << 16); }
__device__ __forceinline__ float hi16(unsigned u) { return __builtin_bit_cast(float, u & 0xffff0000u); }
__device__ __forceinline__ unsigned pk2(float a, float b) { return __builtin_bit_cast(unsigned, __builtin_convertvector(f32x2{a, b}, bf16x2)); }
__device__ __forceinline__ float rbf(float a) { return lo16(pk2(a, a)); }
__device__ __forceinline__ unsigned nos(unsigned u) { return u == SENT ? 0x7FC07FC0u : u; }
__device__ __forceinline__ unsigned nosf(float f) { return nos(__builtin_bit_cast(unsigned, f)); }
__device__ __forceinline__ unsigned tg(float f, unsigned t8) { return __builtin_amdgcn_perm(asu(f), t8, 0x07060500u); }
__device__ __forceinline__ v4u tg4(f32x4 v, unsigned t8) { return v4u{tg(v[0], t8), tg(v[1], t8), tg(v[2], t8), tg(v[3], t8)}; }
__device__ __forceinline__ unsigned untag(v4u& v, unsigned t8) { v ^= t8; return (v[0] | v[1]) | (v[2] | v[3]); }
__device__ __forceinline__ f32x4 mfma(v4u a, v4u b, f32x4 c) {
  return __builtin_amdgcn_mfma_f32_16x16x32_bf16(__builtin_bit_cast(s16x8, a), __builtin_bit_cast(s16x8, b), c, 0, 0, 0);
}
__device__ __forceinline__ f32x4 smfma(v4u a, v8u b, f32x4 c, int idx) {
  return __builtin_amdgcn_smfmac_f32_16x16x64_bf16(__builtin_bit_cast(bf16x8, a), __builtin_bit_cast(bf16x16, b), c, idx, 0, 0);
}
__device__ __forceinline__ v8u cat(v4u a, v4u b) { return __builtin_shufflevector(a, b, 0, 1, 2, 3, 4, 5, 6, 7); }
template <int CTRL> __device__ __forceinline__ float dpp(float x) {
  return __builtin_bit_cast(float, __builtin_amdgcn_mov_dpp(__builtin_bit_cast(int, x), CTRL, 0xf, 0xf, true));
}
constexpr int D_X1 = 0xB1, D_X2 = 0x4E, D_R4 = 0x124, D_R8 = 0x128;
__device__ __forceinline__ float add16(float x) {
  auto s = __builtin_amdgcn_permlane16_swap(asu(x), asu(x), false, false);
  return asf(s[0]) + asf(s[1]);
}
__device__ __forceinline__ float add32(float x) {
  auto s = __builtin_amdgcn_permlane32_swap(asu(x), asu(x), false, false);
  return asf(s[0]) + asf(s[1]);
}
__device__ __forceinline__ f32x2 up2(unsigned u) { return f32x2{lo16(u), hi16(u)}; }
__device__ __forceinline__ float fold32(float a, float b) { auto s = __builtin_amdgcn_permlane32_swap(asu(a), asu(b), false, false); return asf(s[0]) + asf(s[1]); }
__device__ __forceinline__ f32x2 fold32(f32x2 a, f32x2 b) {
  auto s = __builtin_amdgcn_permlane32_swap(asu(a[0]), asu(b[0]), false, false);
  auto t = __builtin_amdgcn_permlane32_swap(asu(a[1]), asu(b[1]), false, false);
  return f32x2{asf(s[0]), asf(t[0])} + f32x2{asf(s[1]), asf(t[1])};
}
__device__ __forceinline__ float fold16(float a, float b) { auto s = __builtin_amdgcn_permlane16_swap(asu(a), asu(b), false, false); return asf(s[0]) + asf(s[1]); }
__device__ __forceinline__ float fold4(f32x4 v) { return fold16(fold32(v[0], v[2]), fold32(v[1], v[3])); }
__device__ __forceinline__ f32x4 asf4(v4u v) { return __builtin_bit_cast(f32x4, v); }
__device__ __forceinline__ float sum_b2_5(float x) { x += dpp<D_R8>(x); x += dpp<D_R4>(x); return add32(add16(x)); }
__device__ __forceinline__ float sum_all(float x) { x += dpp<D_X1>(x); x += dpp<D_X2>(x); return sum_b2_5(x); }

__device__ __forceinline__ rsrc_t mkrsrc(const void* p, int bytes) { return __builtin_amdgcn_make_buffer_rsrc((void*)p, 0, bytes, 0x00020000); }
__device__ __forceinline__ v4u ld16(rsrc_t rs, int off) { return __builtin_amdgcn_raw_buffer_load_b128(rs, off, 0, SC1); }
__device__ __forceinline__ v4u ld16s(rsrc_t rs, int off, int soff) { return __builtin_amdgcn_raw_buffer_load_b128(rs, off, soff, SC1); }
__device__ __forceinline__ v2u ld8(rsrc_t rs, int off) { return __builtin_amdgcn_raw_buffer_load_b64(rs, off, 0, SC1); }
__device__ __forceinline__ unsigned ld4(rsrc_t rs, int off) { return __builtin_amdgcn_raw_buffer_load_b32(rs, off, 0, SC1); }
__device__ __forceinline__ void st16(rsrc_t rs, int off, v4u v) { __builtin_amdgcn_raw_buffer_store_b128(v, rs, off, 0, SC1); }
__device__ __forceinline__ void st8(rsrc_t rs, int off, v2u v) { __builtin_amdgcn_raw_buffer_store_b64(v, rs, off, 0, SC1); }
__device__ __forceinline__ void st4(rsrc_t rs, int off, unsigned v) { __builtin_amdgcn_raw_buffer_store_b32(v, rs, off, 0, SC1); }
__device__ __forceinline__ unsigned um(unsigned a, unsigned b) { return a > b ? a : b; }
__device__ __forceinline__ unsigned um(v4u v) { return um(um(v[0], v[1]), um(v[2], v[3])); }
__device__ __forceinline__ unsigned tagbad(v4u v, unsigned tag) { return (v[1] ^ tag) | (v[3] ^ tag); }
__device__ __forceinline__ float max3(float a, float b, float c) { float d; asm("v_max3_f32 %0, %1, %2, %3" : "=v"(d) : "v"(a), "v"(b), "v"(c)); return d; }
__device__ __forceinline__ float rcp(float x) { return __builtin_amdgcn_rcpf(x); }
__device__ __forceinline__ float dot2(unsigned a, unsigned b, float c) {
  return __builtin_amdgcn_fdot2_f32_bf16(__builtin_bit_cast(bf16x2, a), __builtin_bit_cast(bf16x2, b), c, false);
}
#define FENCE() asm volatile("" ::: "memory")
#define PIN(x) asm volatile("" : "+v"(x))
#define LGKM0() asm volatile("s_waitcnt lgkmcnt(0)" ::: "memory")
#define VM0() asm volatile("s_waitcnt vmcnt(0)" ::: "memory")
#define ROLE_BEGIN() __builtin_amdgcn_s_waitcnt(0x0F70)
#define COUNTDOWN(cnt) do { if (__builtin_expect(--(cnt) == 0, 0)) asm volatile("s_waitcnt vmcnt(0)\n\ts_endpgm" ::: "memory"); } while (0)
#define POLL(BODY) { unsigned sp_ = LIM_GLB; _Pragma("nounroll") for (;;) { FENCE(); bool good; BODY; if (__all(good)) break; COUNTDOWN(sp_); } }

__device__ __forceinline__ unsigned lds_ld(LAS unsigned char* L, int off) { return *(volatile LAS unsigned*)(L + off); }
__device__ __forceinline__ void lds_st(LAS unsigned char* L, int off, unsigned v) { *(volatile LAS unsigned*)(L + off) = v; }
__device__ __forceinline__ void wait_ge(LAS unsigned char* L, int off, unsigned need, unsigned& seen) {
  if (seen >= need) { FENCE(); return; }
  unsigned cnt, tmp, nd, got;
  asm volatile("s_mov_b32 %1, %6\n\tv_readfirstlane_b32 %3, %4\n"
               "1:\n\tds_read_b32 %2, %5\n\ts_waitcnt lgkmcnt(0)\n\tv_readfirstlane_b32 %0, %2\n\ts_cmp_ge_u32 %0, %3\n\ts_cbranch_scc1 2f\n\t"
               "s_sleep 1\n\ts_sub_u32 %1, %1, 1\n\ts_cbranch_scc0 1b\n\ts_waitcnt vmcnt(0)\n\ts_endpgm\n2:"
               : "=&s"(got), "=&s"(cnt), "=&v"(tmp), "=&s"(nd) : "v"(need), "v"((unsigned)(size_t)(L + off)), "i"(LIM_LDS) : "scc", "memory");
  seen = got;
}
__device__ __forceinline__ unsigned min4(LAS unsigned char* L, int off) {
  v4u v = *(volatile LAS v4u*)(L + off);
  return __builtin_amdgcn_readfirstlane(min(min(v[0], v[1]), min(v[2], v[3])));
}
__device__ __forceinline__ v4u trfrag(LAS unsigned char* L, unsigned a) {
  s16x4 x = __builtin_amdgcn_ds_read_tr16_b64_v4i16((LAS s16x4*)(L + a));
  s16x4 y = __builtin_amdgcn_ds_read_tr16_b64_v4i16((LAS s16x4*)(L + a + 4096));
  v2u u = __builtin_bit_cast(v2u, x), v = __builtin_bit_cast(v2u, y);
  return v4u{u[0], u[1], v[0], v[1]};
}
__device__ __forceinline__ f32x4 sel2(bool k, f32x4 a, f32x4 b) {
  return f32x4{k ? b[0] : a[0], k ? b[1] : a[1], k ? b[2] : a[2], k ? b[3] : a[3]};
}

template <int V> struct IC { static constexpr int value = V; };
struct Loader {
  LAS unsigned char* L;
  unsigned cur, pub, cons;
  __device__ __forceinline__ void publish(unsigned v) { if ((int)v > (int)pub) { pub = v; lds_st(L, L_FULL, v); } }
  template <int N> __device__ __forceinline__ void step(rsrc_t rs, int v0, int v1, int v2, int v3, unsigned soff, unsigned s1, unsigned s4, unsigned hs) {
    const unsigned m = hs * HS;
#pragma unroll
    for (int i = 0; i < 8; ++i) {
      const int vo = (i & 3) == 0 ? v0 : (i & 3) == 1 ? v1 : (i & 3) == 2 ? v2 : v3;
      asm volatile("s_mov_b32 m0, %0\n\ts_nop 0\n\tbuffer_load_dwordx4 %1, %2, %3 offen nt lds\n\ts_waitcnt vmcnt(%4)"
                   :: "s"(m + 1024u * i), "v"(vo), "s"(rs), "s"(soff + s1 * (i & 3) + s4 * (i >> 2)), "i"(N - 1) : "memory");
      if (((i - N - 6) & 7) == 0) publish(cur + 1 + (i - N - 6) / 8);
    }
    ++cur;
  }
  template <int N> __device__ __forceinline__ void tile(rsrc_t rs, int v0, int v1, unsigned soff, unsigned pitch, unsigned hs) {
    step<N>(rs, v0, v1, v0, v1, soff, 4 * pitch, 16 * pitch, hs);
  }
  __device__ __forceinline__ void drain() {
    asm volatile("s_waitcnt vmcnt(24)" ::: "memory"); publish(cur - 3);
    asm volatile("s_waitcnt vmcnt(16)" ::: "memory"); publish(cur - 2);
    asm volatile("s_waitcnt vmcnt(8)" ::: "memory"); publish(cur - 1);
    VM0(); publish(cur);
  }
  __device__ __forceinline__ void need(int off, unsigned v) {
    if (cons >= v) return;
    cons = min4(L, off);
    if (cons >= v) return;
    drain();
    unsigned sp = LIM_LDS;
#pragma nounroll
    for (;;) {
      cons = min4(L, off);
      if (cons >= v) break;
      COUNTDOWN(sp);
      __builtin_amdgcn_s_sleep(1);
    }
  }
};

__global__ void __launch_bounds__(384, 2) fused_decode_block(Params p) {
  __shared__ __attribute__((aligned(1024))) unsigned char lds_raw[LDS_BYTES];
  LAS unsigned char* L = (LAS unsigned char*)lds_raw;
  const int tid = threadIdx.x, lane = tid & 63, wave = __builtin_amdgcn_readfirstlane(tid >> 6);
  unsigned long long pc0;
  asm volatile("s_getpc_b64 %0" : "=s"(pc0));
  const int cu = blockIdx.x, xcd = cu & 7, r = cu >> 3;
  const int rm1 = (r + 1) & 31;
  const bool isP = r < 16;
  const int ph = 4 * xcd + (r >> 2), nb = r & 3;
  const int a1 = 2 * xcd + ((r >> 3) & 1), b1 = r & 7;
  const int kvh = r & 3, sp = (isP ? 0 : 32) + 4 * xcd + ((r & 15) >> 2);
  const int blk0 = NBLK * sp, nw = isP ? NWP : NWQ;
  const int g = lane >> 4, c = lane & 15;
  unsigned char* const tb = (unsigned char*)(((uintptr_t)p.ws + 0x1FFFFF) & ~(uintptr_t)0x1FFFFF);
  const rsrc_t rs = mkrsrc(tb, T_END);
  auto o_pa = [&](int kv, int s_, int h) { return T_PA + ((kv & 1) * 16 + (s_ >> 2)) * 16384 + h * 8192 + (s_ & 3) * 2048; };
  auto o_pw = [&](int kv, int s_, int h) { return T_PW + (kv & 1) * 16384 + h * 8192 + s_ * 128; };
  unsigned cin, xs;
  v4u xa, xb;
  if (wave == 5) {
    const v4u S = {SENT, SENT, SENT, SENT};
    cin = ld4(rs, WS_EPOCH);
    st16(rs, o_pw(kvh, sp, kvh >> 1) + lane * 16 + (lane >> 3) * (OOB >> 3), S);
    st16(rs, o_pa(kvh, sp, kvh >> 1) + lane * 16, S);
    st16(rs, o_pa(kvh, sp, kvh >> 1) + 1024 + lane * 16, S);
  } else if (wave != 0) {
    cin = *(const unsigned*)(p.c + 128 * xcd + 2 * lane);
    xa = *(const v4u*)(p.x + 8 * lane); xb = *(const v4u*)(p.x + 512 + 8 * lane);
    xs = *(const unsigned*)(p.x + 128 * xcd + 32 * (wave - 1) + 2 * c);
    if (wave == 1) lds_st(L, SCR + 4 * lane, 0u);
  }
  LGKM0();
  __builtin_amdgcn_s_barrier();

  if (__builtin_expect(wave == 0, 1)) {
    ROLE_BEGIN();
    __builtin_amdgcn_s_setprio(3);
    Loader ld{L, 0u, 0u, 0u};
    const int c0 = c ^ (2 * g), c1 = c ^ (2 * g + 8);
    const rsrc_t rm = mkrsrc(p.wmod, 1024 * 6144 * 2);
    const unsigned mb = 128 * xcd * 12288;
    {
      const int v0 = (g + 32 * (c0 >> 3)) * 12288 + (c0 & 7) * 16, v1 = (g + 32 * (c1 >> 3)) * 12288 + (c1 & 7) * 16;
#pragma nounroll
      for (int s = 0; s < 2; ++s) ld.tile<WIN0>(rm, v0, v1, mb + s * 64 * 12288 + 128 * rm1, 12288, s);
    }
    {
      const rsrc_t rq = mkrsrc(p.wq, 1024 * 4096 * 2);
      const int v0 = g * 8192 + c0 * 16, v1 = g * 8192 + c1 * 16;
      const unsigned qb = 128 * xcd * 8192 + 256 * r;
#pragma nounroll
      for (int s = 0; s < 2; ++s) ld.tile<WIN0>(rq, v0, v1, qb + s * 32 * 8192, 8192, 2 + s);
      asm volatile("s_waitcnt vmcnt(16)" ::: "memory"); ld.publish(2);
#pragma nounroll
      for (int s = 2; s < 4; ++s) ld.tile<WIN>(rq, v0, v1, qb + s * 32 * 8192, 8192, 2 + s);
    }
    {
      const int v0 = g * 12288 + c0 * 16, v1 = g * 12288 + c1 * 16;
#pragma nounroll
      for (int s = 0; s < 4; ++s) ld.tile<WIN>(rm, v0, v1, mb + s * 32 * 12288 + 4096 + 256 * rm1, 12288, 6 + s);
    }
    const int pitch = isP ? 2048 : 7168;
    const int vw0 = g * pitch + c0 * 16, vw1 = g * pitch + c1 * 16;
    const int vl0 = (g + 32 * (c0 >> 3)) * 7168 + (c0 & 7) * 16, vl1 = (g + 32 * (c1 >> 3)) * 7168 + (c1 & 7) * 16;
    auto gu_tile = [&](rsrc_t rw, unsigned hs0, auto win) {
      const unsigned wb = 64 * a1 * 7168 + 896 * b1;
#pragma nounroll
      for (int j = 0; j < 6; ++j) ld.tile<decltype(win)::value>(rw, vw0, vw1, wb + (j & 1) * 32 * 7168 + 256 * (j >> 1), 7168, hs0 + j);
      ld.tile<decltype(win)::value>(rw, vl0, vl1, wb + 768, 7168, hs0 + 6);
    };
    if (isP) {
      const rsrc_t ro = mkrsrc(p.wo, 4096 * 1024 * 2);
#pragma nounroll
      for (int j = 0; j < 8; ++j) ld.tile<WIN>(ro, vw0, vw1, (128 * ph + 32 * (j >> 1)) * 2048 + 512 * nb + 256 * (j & 1), 2048, HEAD + j);
      ld.step<WIN>(mkrsrc(p.wd, 3584 * 1024 * 2), vw0, vw1, vw0, vw1, (112 * ph + 96) * 2048 + 512 * nb, 4 * 2048, 256, 18);
    } else gu_tile(mkrsrc(p.wg, 1024 * 3584 * 2), HEAD, IC<WIN>{});
    const rsrc_t rk = mkrsrc(p.K + (size_t)kvh * 65536 * 128, 65536 * 256), rv = mkrsrc(p.V + (size_t)kvh * 65536 * 128, 65536 * 256);
    const int vk0 = g * 256 + (c ^ g) * 16, vk1 = g * 256 + (c ^ (g + 4)) * 16, vk2 = g * 256 + (c ^ (g + 8)) * 16, vk3 = g * 256 + (c ^ (g + 12)) * 16;
    const int vv0 = g * 256 + c0 * 16, vv1 = g * 256 + c1 * 16;
    unsigned hs = isP ? (HEAD + NWP) % NHS : HEAD + NWQ;
#pragma nounroll
    for (int n = 0; n < 2 * NBLK; ++n) {
      ld.need(L_CONS, max((int)ld.cur - NHS + 1, 0));
      if (n == 0 || ((n & 1) && n != 2 * NBLK - 1)) ld.step<WIN>(rk, vk0, vk1, vk2, vk3, (unsigned)(blk0 + ((n + 1) >> 1)) * 8192u, 1024, 4096, hs);
      else ld.tile<WIN>(rv, vv0, vv1, (unsigned)(blk0 + ((n - 1) >> 1)) * 8192u, 256, hs);
      if (++hs == NHS) hs = 0;
    }
    if (isP) {
      const rsrc_t rd = mkrsrc(p.wd, 3584 * 1024 * 2);
      const unsigned db = 112 * ph * 2048 + 512 * nb;
      ld.need(L_CONS, ld.cur + 7 - NHS);
#pragma nounroll
      for (int j = 0; j < NSEAM; ++j) ld.tile<WIN>(rd, vw0, vw1, db + 32 * (j >> 1) * 2048 + 256 * (j & 1), 2048, DN0 + j);
      ld.drain();
      __builtin_amdgcn_s_barrier();
      __builtin_amdgcn_s_barrier();
#pragma nounroll
      for (int j = NSEAM; j < 6; ++j) {
        if (j == NSEAM + NPRE) {
          unsigned sp = LIM_LDS;
          while (min4(L, L_CONS) < 1000u) { COUNTDOWN(sp); __builtin_amdgcn_s_sleep(1); }
        }
        ld.tile<WINP>(rd, vw0, vw1, db + 32 * (j >> 1) * 2048 + 256 * (j & 1), 2048, DN0 + j);
      }
    } else {
      ld.need(L_CONS, ld.cur + 7 - NHS);
      gu_tile(mkrsrc(p.wu, 1024 * 3584 * 2), UP0, IC<WINQ>{});
    }
    ld.drain();
    return;
  }

  if (wave == 5) {
    PIN(cin);
    const unsigned tag = cin + 1u;
    const int qi = 8 * xcd + r;
    VM0();
    lds_st(L, L_TAG, tag);
    lds_st(L, L_CLR, 1u);
    if (cu == 120) {
      unsigned m0 = ~0u, m1 = ~0u;
#pragma nounroll
      for (int k = 0; k < 8; ++k) {
        unsigned long long ta, tc; unsigned d;
        asm volatile("s_memtime %0\n\ts_waitcnt lgkmcnt(0)\n\ts_load_dword %2, %3, 0\n\ts_waitcnt lgkmcnt(0)\n\ts_memtime %1\n\ts_waitcnt lgkmcnt(0)"
                     : "=&s"(ta), "=&s"(tc), "=&s"(d) : "s"(tb + T_PROBE + (k & 1) * 8192 + (k >> 1) * 16384) : "memory");
        if (k & 1) m1 = min(m1, (unsigned)(tc - ta)); else m0 = min(m0, (unsigned)(tc - ta));
      }
      st8(rs, lane == 0 ? WS_PI : OOB, v2u{m1 < m0 ? 1u : 0u, tag});
    }
    unsigned seen = 0;
    wait_ge(L, L_SYNC, 8, seen);
    if (r < 8) {
      v4u v[4];
      const int off = WS_Q1 + (lane >> 5) * 32768 + 512 * qi + 16 * (lane & 31);
      __builtin_amdgcn_s_sleep(RSLEEP);
      POLL({ _Pragma("unroll") for (int k = 0; k < 4; ++k) v[k] = ld16(rs, off + k * 65536);
             good = (tagbad(v[0], tag) | tagbad(v[1], tag) | tagbad(v[2], tag) | tagbad(v[3], tag)) == 0; });
      float s[2];
#pragma unroll
      for (int e = 0; e < 2; ++e) s[e] = add32((asf(v[0][2 * e]) + asf(v[1][2 * e])) + (asf(v[2][2 * e]) + asf(v[3][2 * e])));
      st8(rs, WS_QF + (lane >> 5) * 16384 + 256 * qi + 8 * (lane & 31), v2u{pk2(s[0], s[1]), tag});
    }
    v4u qv[4]; v2u pv;
    if (r < 8) __builtin_amdgcn_s_sleep(QSLEEP1); else if (cu != 120) __builtin_amdgcn_s_sleep(QSLEEP2);
    POLL({ _Pragma("unroll") for (int k = 0; k < 4; ++k) qv[k] = ld16(rs, WS_QF + (xcd & 1) * 16384 + kvh * 4096 + 1024 * k + 16 * lane);
           pv = ld8(rs, WS_PI);
           good = (tagbad(qv[0], tag) | tagbad(qv[1], tag) | tagbad(qv[2], tag) | tagbad(qv[3], tag) | (pv[1] ^ tag)) == 0; });
#pragma unroll
    for (int k = 0; k < 4; ++k) *(LAS v2u*)(L + L_Q + 512 * k + 8 * lane) = v2u{qv[k][0], qv[k][2]};
    LGKM0();
    lds_st(L, L_QRDY, 1u + (pv[0] & 1u));
    if (cu == 0) st4(rs, lane == 0 ? WS_EPOCH : OOB, tag);
    if (!isP) {
      v4u v[8];
      const int off = WS_M2 + g * 16384 + 256 * a1 + 16 * c;
      POLL({ unsigned mx = 0;
             _Pragma("unroll") for (int k = 0; k < 8; ++k) v[k] = ld16(rs, off + (k & 1) * 65536 + (k >> 1) * 4096);
             _Pragma("unroll") for (int k = 0; k < 8; ++k) mx |= untag(v[k], tag & 255u);
             good = (mx & 255u) == 0; });
#pragma unroll
      for (int sec = 0; sec < 4; ++sec) {
        f32x4 t;
#pragma unroll
        for (int e = 0; e < 4; ++e) t[e] = rbf(add32(add16(asf(v[2 * sec][e]) + asf(v[2 * sec + 1][e]))));
        if (g == 0) *(LAS f32x4*)(L + L_MOD + 256 * sec + 16 * c) = t;
      }
      LGKM0();
      lds_st(L, L_MRDY, 1u);
    }
    {
      unsigned long long pc1;
      asm volatile("s_getpc_b64 %0" : "=s"(pc1));
      const v4u t = __builtin_amdgcn_raw_buffer_load_b128(mkrsrc((const void*)(pc0 & ~127ull), pc1 > pc0 ? (int)(pc1 - pc0) : 0), 1024 * r + 16 * lane, 0, 0);
      asm volatile("" :: "v"(t));
    }
    return;
  }

  const int w = wave - 1;
  const unsigned FP = 64 * g + 16 * ((c >> 1) & 3) + 8 * (c >> 3) + 4 * (c & 1);
  LAS unsigned char* PV = L + L_PRIV + 256 * w;
  v4u sf[4];
  {
    const float v0 = lo16(cin), v1 = hi16(cin);
    *(LAS unsigned*)(PV + FP) = pk2(v0 / (1.f + expf(-v0)), v1 / (1.f + expf(-v1)));
    LGKM0();
#pragma unroll
    for (int ks = 0; ks < 4; ++ks) sf[ks] = *(LAS v4u*)(PV + 64 * ks + 16 * g);
  }
  float ln1[2];
  {
    float s = 0.f;
#pragma unroll
    for (int k = 0; k < 4; ++k) s += (lo16(xa[k]) + hi16(xa[k])) + (lo16(xb[k]) + hi16(xb[k]));
    const float mean = sum_all(s) * (1.f / 1024.f);
    float q = 0.f;
#pragma unroll
    for (int k = 0; k < 4; ++k) {
      const float d0 = lo16(xa[k]) - mean, d1 = hi16(xa[k]) - mean, d2 = lo16(xb[k]) - mean, d3 = hi16(xb[k]) - mean;
      q += (d0 * d0 + d1 * d1) + (d2 * d2 + d3 * d3);
    }
    const float var = sum_all(q) * (1.f / 1024.f);
    const float m = rbf(mean), den = rbf(sqrtf(rbf(rbf(var) + 1e-5f)));
    ln1[0] = rbf(rbf(lo16(xs) - m) / den); ln1[1] = rbf(rbf(hi16(xs) - m) / den);
  }
  const unsigned A0 = (4 * g + (c >> 2)) * 256 + 32 * ((4 * g + (c >> 2)) & 7) + 8 * (c & 3);
  const unsigned At0 = A0 ^ (64 * w), At1 = A0 ^ (64 * w + 32);
  unsigned full = 0, seen = 0, tag;
  const f32x4 Z = {0.f, 0.f, 0.f, 0.f};
  wait_ge(L, L_FULL, 2, full);
  {
    f32x4 d = Z;
#pragma unroll
    for (int ks = 0; ks < 4; ++ks) d = mfma(trfrag(L, (A0 ^ (128 * (ks & 1) + 32 * w)) + (ks >> 1) * HS), sf[ks], d);
    wait_ge(L, L_CLR, 1, seen);
    tag = lds_ld(L, L_TAG);
    st16(rs, c < 2 ? WS_M1 + (rm1 * 8 + xcd) * 512 + 128 * w + 32 * g + 16 * c : OOB, v4u{asu(c ? d[2] : d[0]), tag, asu(c ? d[3] : d[1]), tag});
  }
  wait_ge(L, L_FULL, 6, full);
  v4u wf[8];
  {
#pragma unroll
    for (int ks = 0; ks < 4; ++ks) { wf[ks] = trfrag(L, At0 + (2 + ks) * HS); wf[4 + ks] = trfrag(L, At1 + (2 + ks) * HS); }
    LGKM0();
    lds_st(L, L_CONS + 4 * w, 6u);
  }
  {
    v4u v[4];
    const int off = WS_M1 + ((2 * xcd + (w >> 1)) * 8 + g) * 512 + 256 * (w & 1) + 16 * c;
    __builtin_amdgcn_s_sleep(ESLEEP);
    POLL({ _Pragma("unroll") for (int k = 0; k < 4; ++k) v[k] = ld16(rs, off + (k & 1) * 2048 + (k >> 1) * 65536);
           good = (tagbad(v[0], tag) | tagbad(v[1], tag) | tagbad(v[2], tag) | tagbad(v[3], tag)) == 0; });
    float h[2];
#pragma unroll
    for (int k = 0; k < 2; ++k) {
      const float s = rbf(add32(add16(asf(v[0][2 * k]) + asf(v[1][2 * k])))), cc = rbf(add32(add16(asf(v[2][2 * k]) + asf(v[3][2 * k]))));
      h[k] = rbf(ln1[k] * rbf(1.f + cc)) + s;
    }
    if (g == 0) *(LAS unsigned*)(L + L_H + 64 * w + FP) = pk2(h[0], h[1]);
    LGKM0();
    if (lane == 0) __hip_atomic_fetch_add((LAS unsigned*)(L + L_SYNC), 1u, __ATOMIC_RELAXED, __HIP_MEMORY_SCOPE_WORKGROUP);
    seen = 0;
    wait_ge(L, L_SYNC, 4, seen);
    f32x4 d0 = Z, d1 = Z;
#pragma unroll
    for (int ks = 0; ks < 4; ++ks) { const v4u hf = *(LAS v4u*)(L + L_H + 64 * ks + 16 * g); d0 = mfma(wf[ks], hf, d0); d1 = mfma(wf[4 + ks], hf, d1); }
    const f32x4 d = sel2(c & 2, d0, d1);
    st16(rs, c < 4 ? WS_Q1 + xcd * 32768 + 1024 * r + 256 * w + 128 * (c >> 1) + 32 * g + 16 * (c & 1) : OOB,
         v4u{asu((c & 1) ? d[2] : d[0]), tag, asu((c & 1) ? d[3] : d[1]), tag});
  }
  if (lane == 0) __hip_atomic_fetch_add((LAS unsigned*)(L + L_SYNC), 1u, __ATOMIC_RELAXED, __HIP_MEMORY_SCOPE_WORKGROUP);
  wait_ge(L, L_FULL, 10, full);
  {
    f32x4 d0 = Z, d1 = Z;
#pragma unroll
    for (int ks = 0; ks < 4; ++ks) { d0 = mfma(trfrag(L, At0 + (6 + ks) * HS), sf[ks], d0); d1 = mfma(trfrag(L, At1 + (6 + ks) * HS), sf[ks], d1); }
    LGKM0();
    lds_st(L, L_CONS + 4 * w, 10u);
    st16(rs, c < 2 ? WS_M2 + xcd * 16384 + 512 * rm1 + 128 * w + 64 * c + 16 * g : OOB, tg4(sel2(c != 0, d0, d1), tag & 255u));
  }
  auto gu_addr = [&](int t, int ks, unsigned hs0) {
    const int T = 7 * w + t;
    return (A0 ^ (T < 24 ? 32 * (T & 7) : 32 * (4 * ks + T - 24))) + (hs0 + (T < 24 ? 2 * (T >> 3) + ks : 6)) * HS;
  };
  v8u w8[14];
  v2u wh[4];
  wait_ge(L, L_FULL, HEAD + nw, full);
  if (isP) {
#pragma unroll
    for (int t = 0; t < 4; ++t) {
      const unsigned ad = (A0 ^ (128 * (w & 1) + 32 * t)) + (HEAD + (w >> 1)) * HS;
      w8[2 * t] = cat(trfrag(L, ad), trfrag(L, ad + 2 * HS)); w8[2 * t + 1] = cat(trfrag(L, ad + 4 * HS), trfrag(L, ad + 6 * HS));
      wh[t] = __builtin_bit_cast(v2u, __builtin_amdgcn_ds_read_tr16_b64_v4i16((LAS s16x4*)(L + (A0 ^ (128 * (w & 1) + 32 * t)) + 18 * HS + 4096 * (w >> 1))));
    }
  } else {
#pragma unroll
    for (int t = 0; t < 7; ++t) w8[t] = cat(trfrag(L, gu_addr(t, 0, HEAD)), trfrag(L, gu_addr(t, 1, HEAD)));
  }
  LGKM0();
  lds_st(L, L_CONS + 4 * w, HEAD + nw);
  unsigned hs = isP ? (HEAD + NWP) % NHS : HEAD + NWQ, st = HEAD + nw;
  const unsigned K0 = c * 256 + 16 * (g ^ (c & 3)) + 16 * (c & 12);
  v4u kf[8];
  auto k_reads = [&](unsigned kb) {
#pragma unroll
    for (int s = 0; s < 4; ++s) {
      kf[s] = *(LAS v4u*)(L + kb + (K0 ^ (64 * s)));
      kf[4 + s] = *(LAS v4u*)(L + kb + (K0 ^ (64 * s)) + 4096);
    }
  };
  unsigned hb = hs * HS;
  auto next = [&]() { const unsigned h = hb; hb = hb == (NHS - 1) * HS ? 0 : hb + HS; return h; };
  wait_ge(L, L_FULL, ++st, full);
  k_reads(next());
  lds_st(L, L_CONS + 4 * w, st);
  v4u qf[4];
  seen = 0;
  wait_ge(L, L_QRDY, 1, seen);
  const int pi = seen - 1;
  full = __builtin_amdgcn_readfirstlane(lds_ld(L, L_FULL));
#pragma unroll
  for (int s = 0; s < 4; ++s) qf[s] = *(LAS v4u*)(L + L_Q + (c & 7) * 256 + 64 * s + 16 * g);
  const unsigned t8 = tag & 255u;
  const int own = (((xcd >> 2) ^ pi) & 1) * 8192;
  const int sidx = (c & 1) ? 0xEEEE : 0x4444;
  auto spos = [](int s_) { return 128 * (s_ >> 6) + 64 * ((s_ >> 5) & 1) + 32 * ((s_ >> 3) & 1) + 16 * ((s_ >> 1) & 1) + 8 * ((s_ >> 2) & 1) + 4 * ((s_ >> 4) & 1); };
  int o1_ = o_pa(ph >> 3, 16 * w + g, 0) + own + (ph & 7) * 256 + 16 * c, o2_ = o_pw(ph >> 3, 16 * w + g, 0) + own + 4 * (ph & 7);
  int so1 = (c & 3) == 0 ? T_P1 + (ph >> 2) * 16384 + ((nb >> 1) ^ pi) * 8192 + (ph & 3) * 2048 + (nb & 1) * 1024 + 256 * w + 64 * g + 4 * c : OOB;
  int oab = T_AB + 4 * w * 65536 + ((ph >> 2) & 3) * 16384 + (own ^ 8192) + (ph & 3) * 2048 + 16 * lane;
  unsigned xa_[4], sp2 = L_PRIV + 256 * w + spos(2 * lane);
  unsigned ra = L_REC + 512 * w + 32 * c + 16 * (g & 1) + 8 * (g >> 1), rb_ = L_REC + 8 * lane;
#pragma unroll
  for (int t = 0; t < 4; ++t) { xa_[t] = (c >> 1) == 2 * t ? L_PRIV + 256 * w + 32 * g + 16 * (c & 1) : L_ZERO; PIN(xa_[t]); }
  PIN(o1_); PIN(o2_); PIN(so1); PIN(sp2); PIN(oab); PIN(ra); PIN(rb_);
  f32x4 o0 = Z, o1 = Z;
  float m = -INFINITY, l = 0.f;
  {
    int so0 = o_pa(kvh, sp, (kvh >> 1) ^ pi) + (c & 7) * 256 + 64 * w + 32 * (c >> 3) + 8 * g + ((g & 1) ? OOB : 0);
    PIN(so0);
    v4u v0, v1, pf;
    f32x4 s0, s1;
    auto scores = [&]() {
      s0 = Z; s1 = Z;
#pragma unroll
      for (int s = 0; s < 4; ++s) { s0 = mfma(kf[s], qf[s], s0); s1 = mfma(kf[4 + s], qf[s], s1); }
    };
    float alast = 1.f;
    auto softmax = [&](auto hold) {
      const f32x4 sv = sel2(c >= 8, s0, s1);
      const unsigned r01 = pk2(sv[0], sv[1]), r23 = pk2(sv[2], sv[3]);
      constexpr float SCALE = 0.08838834764831845f;
      const f32x2 y01 = up2(r01) * SCALE, y23 = up2(r23) * SCALE;
      f32x2 x01 = up2(pk2(y01[0], y01[1])), x23 = up2(pk2(y23[0], y23[1]));
      float mx = max3(max3(x01[0], x01[1], x23[0]), x23[1], m);
      if (__any(mx != m)) {
        mx = max3(mx, mx, dpp<D_R8>(mx));
        { auto t = __builtin_amdgcn_permlane16_swap(asu(mx), asu(mx), false, false); mx = max3(mx, asf(t[0]), asf(t[1])); }
        { auto t = __builtin_amdgcn_permlane32_swap(asu(mx), asu(mx), false, false); mx = max3(mx, asf(t[0]), asf(t[1])); }
        const float al = __builtin_amdgcn_exp2f((m - mx) * L2E);
        m = mx; l *= al;
        if (decltype(hold)::value) alast = al; else { o0 *= al; o1 *= al; }
      }
      x01 = (x01 - m) * L2E; x23 = (x23 - m) * L2E;
      const float p0 = __builtin_amdgcn_exp2f(x01[0]), p1 = __builtin_amdgcn_exp2f(x01[1]), p2 = __builtin_amdgcn_exp2f(x23[0]), p3 = __builtin_amdgcn_exp2f(x23[1]);
      l += (p0 + p1) + (p2 + p3);
      const int pa = (int)pk2(p0, p1), pb = (int)pk2(p2, p3);
      pf = v4u{(unsigned)__builtin_amdgcn_update_dpp(pa, pa, D_R8, 0xf, 0xC, false), (unsigned)__builtin_amdgcn_update_dpp(pb, pb, D_R8, 0xf, 0xC, false),
               (unsigned)__builtin_amdgcn_update_dpp(pa, pa, D_R8, 0xf, 0x3, false), (unsigned)__builtin_amdgcn_update_dpp(pb, pb, D_R8, 0xf, 0x3, false)};
    };
#pragma nounroll
    for (int i = NBLK - 1; i > 1; --i) {
      const unsigned kb = next(), vb = next();
      st += 2;
      __builtin_amdgcn_sched_barrier(0);
      scores();
      __builtin_amdgcn_sched_barrier(0);
      const bool early = full >= st;
      if (early) { v0 = trfrag(L, vb + At0); v1 = trfrag(L, vb + At1); k_reads(kb); lds_st(L, L_CONS + 4 * w, st); }
      const unsigned peek = lds_ld(L, L_FULL);
      __builtin_amdgcn_sched_barrier(0);
      softmax(IC<0>{});
      __builtin_amdgcn_sched_barrier(0);
      full = __builtin_amdgcn_readfirstlane(peek);
      if (!early) {
        wait_ge(L, L_FULL, st, full);
        v0 = trfrag(L, vb + At0); v1 = trfrag(L, vb + At1); k_reads(kb);
        lds_st(L, L_CONS + 4 * w, st);
      }
      __builtin_amdgcn_sched_barrier(0);
      o0 = mfma(v0, pf, o0); o1 = mfma(v1, pf, o1);
    }
    const unsigned kb = next(), vb = next();
    scores();
    softmax(IC<0>{});
    const v4u pfa = pf;
    wait_ge(L, L_FULL, st + 1, full);
    k_reads(kb);
    scores();
    softmax(IC<1>{});
    l += dpp<D_R8>(l);
    l = add32(add16(l));
    const float inv = rcp(l);
    if (w == 0) st4(rs, lane < 8 ? o_pw(kvh, sp, (kvh >> 1) ^ pi) + 4 * lane : OOB, nosf(m * L2E + __builtin_amdgcn_logf(l)));
    float fa = alast * inv;
    PIN(fa);
    wait_ge(L, L_FULL, st + 2, full);
    v0 = trfrag(L, vb + At0); v1 = trfrag(L, vb + At1);
    o0 = mfma(v0, pfa, o0); o1 = mfma(v1, pfa, o1);
    f32x4 oa = sel2(c >= 8, o0, o1) * fa;
    PIN(oa);
    st += 3;
    wait_ge(L, L_FULL, st, full);
    v0 = trfrag(L, hb + At0); v1 = trfrag(L, hb + At1);
    const f32x4 ov = __builtin_elementwise_fma(sel2(c >= 8, mfma(v0, pf, Z), mfma(v1, pf, Z)), f32x4{inv, inv, inv, inv}, oa);
    {
      const unsigned a0 = nos(pk2(ov[0], ov[1])), a1_ = nos(pk2(ov[2], ov[3]));
      auto s0_ = __builtin_amdgcn_permlane16_swap(a0, a0, false, false);
      auto s1_ = __builtin_amdgcn_permlane16_swap(a1_, a1_, false, false);
      st16(rs, so0, v4u{s0_[0], s1_[0], s0_[1], s1_[1]});
    }
    lds_st(L, L_CONS + 4 * w, st);
  }
  if (isP) {
    v4u v[4]; unsigned wl[4];
    auto gemv = [&](int so) {
      v4u xf[8];
#pragma unroll
      for (int f = 0; f < 8; ++f) xf[f] = *(LAS v4u*)(L + xa_[f >> 1] + 128 * (f & 1));
      __builtin_amdgcn_sched_barrier(0);
      f32x4 d = Z;
#pragma unroll
      for (int f = 0; f < 8; ++f) d = smfma(xf[f], w8[f], d, sidx);
      const float y = d[0] + d[1];
      st16(rs, so, tg4(f32x4{y, dpp<0x55>(y), dpp<0xAA>(y), dpp<0xFF>(y)}, t8));
    };
    POLL({ _Pragma("unroll") for (int k = 0; k < 4; ++k) wl[k] = __builtin_amdgcn_raw_buffer_load_b32(rs, o2_ + 512 * k, 0, SC1);
           good = um(um(wl[0], wl[1]), um(wl[2], wl[3])) != SENT; });
    v4u wa[4];
    {
      float W = max3(max3(asf(wl[0]), asf(wl[1]), asf(wl[2])), asf(wl[3]), asf(wl[3]));
      { auto t = __builtin_amdgcn_permlane16_swap(asu(W), asu(W), false, false); W = max3(W, asf(t[0]), asf(t[1])); }
      { auto t = __builtin_amdgcn_permlane32_swap(asu(W), asu(W), false, false); W = max3(W, asf(t[0]), asf(t[1])); }
      const float den = add32(add16((__builtin_amdgcn_exp2f(asf(wl[0]) - W) + __builtin_amdgcn_exp2f(asf(wl[1]) - W)) +
                                    (__builtin_amdgcn_exp2f(asf(wl[2]) - W) + __builtin_amdgcn_exp2f(asf(wl[3]) - W))));
      if (lane == 0) *(LAS f32x2*)(L + L_ST + 8 * w) = f32x2{W, den};
      FENCE();
#pragma unroll
      for (int k = 0; k < 4; ++k) v[k] = ld16s(rs, o1_, 16384 * k);
      LGKM0();
      __builtin_amdgcn_s_barrier();
      FENCE();
      const f32x4 s01 = *(LAS f32x4*)(L + L_ST), s23 = *(LAS f32x4*)(L + L_ST + 16);
      const float WW = max3(max3(s01[0], s01[2], s23[0]), s23[2], s23[2]);
      const float dd = (__builtin_amdgcn_exp2f(s01[0] - WW) * s01[1] + __builtin_amdgcn_exp2f(s01[2] - WW) * s01[3]) +
                       (__builtin_amdgcn_exp2f(s23[0] - WW) * s23[1] + __builtin_amdgcn_exp2f(s23[2] - WW) * s23[3]);
      const float inv = rcp(dd);
      const unsigned hm = (c & 1) ? 0xFFFF0000u : 0x0000FFFFu;
#pragma unroll
      for (int k = 0; k < 4; ++k) {
        const float wt = __builtin_amdgcn_exp2f(asf(wl[k]) - WW) * inv;
        const float x = c < 8 ? wt : wt - rbf(wt);
        const unsigned u = pk2(x, x) & hm;
#pragma unroll
        for (int d = 0; d < 4; ++d) wa[k][d] = ((c & 7) >> 1) == d ? u : 0u;
        PIN(wa[k]);
      }
    }
    if (!__all(um(um(um(v[0]), um(v[1])), um(um(v[2]), um(v[3]))) != SENT))
      POLL({ _Pragma("unroll") for (int k = 0; k < 4; ++k) v[k] = ld16s(rs, o1_, 16384 * k);
             good = um(um(um(v[0]), um(v[1])), um(um(v[2]), um(v[3]))) != SENT; });
    v4u ug[4];
#pragma unroll
    for (int k = 0; k < 4; ++k) ug[k] = ld16s(rs, oab, k * 65536);
    FENCE();
    {
      f32x4 d = Z;
#pragma unroll
      for (int k = 0; k < 4; ++k) d = mfma(wa[k], v[k], d);
      *(LAS f32x2*)(L + ra) = fold32(f32x2{d[0], d[1]}, f32x2{d[2], d[3]});
    }
    LGKM0();
    __builtin_amdgcn_s_barrier();
    FENCE();
    {
      const unsigned o = rb_;
      const f32x2 e = (*(LAS f32x2*)(L + o) + *(LAS f32x2*)(L + o + 512)) + (*(LAS f32x2*)(L + o + 1024) + *(LAS f32x2*)(L + o + 1536));
      *(LAS unsigned*)(L + sp2) = pk2(e[0], e[1]);
    }
    LGKM0();
    gemv(so1);
    __builtin_amdgcn_sched_barrier(0);
#pragma unroll
    for (int k = 0; k < 4; ++k) PIN(ug[k]);
    lds_st(L, L_CONS + 4 * w, 1000u);
    {
      if (!__all((((untag(ug[0], t8) | untag(ug[1], t8)) | (untag(ug[2], t8) | untag(ug[3], t8))) & 255u) == 0))
        POLL({ _Pragma("unroll") for (int k = 0; k < 4; ++k) ug[k] = ld16s(rs, oab, k * 65536);
               good = (((untag(ug[0], t8) | untag(ug[1], t8)) | (untag(ug[2], t8) | untag(ug[3], t8))) & 255u) == 0; });
      *(LAS f32x4*)(L + L_ABP + 2048 * w + 16 * lane) = (asf4(ug[0]) + asf4(ug[1])) + (asf4(ug[2]) + asf4(ug[3]));
      __builtin_amdgcn_s_sleep(ABSLEEP);
      POLL({ _Pragma("unroll") for (int k = 0; k < 4; ++k) ug[k] = ld16s(rs, oab, 1024 + k * 65536);
             FENCE();
             good = (((untag(ug[0], t8) | untag(ug[1], t8)) | (untag(ug[2], t8) | untag(ug[3], t8))) & 255u) == 0; });
      *(LAS f32x4*)(L + L_ABP + 2048 * w + 1024 + 16 * lane) = (asf4(ug[0]) + asf4(ug[1])) + (asf4(ug[2]) + asf4(ug[3]));
    }
    LGKM0();
    if (lane == 0) __hip_atomic_fetch_add((LAS unsigned*)(L + L_SYNC), 1u, __ATOMIC_RELAXED, __HIP_MEMORY_SCOPE_WORKGROUP);
    seen = 0;
    wait_ge(L, L_SYNC, 12, seen);
    f32x4 abg, abu;
    { const int o = L_ABP + 16 * lane;
      abg = (*(LAS f32x4*)(L + o) + *(LAS f32x4*)(L + o + 2048)) + (*(LAS f32x4*)(L + o + 4096) + *(LAS f32x4*)(L + o + 6144));
      abu = (*(LAS f32x4*)(L + o + 1024) + *(LAS f32x4*)(L + o + 3072)) + (*(LAS f32x4*)(L + o + 5120) + *(LAS f32x4*)(L + o + 7168)); }
    unsigned h0a = L_PRIV + 256 * w + spos(lane & ~1) + 2 * (c & 1), h1a = h0a + 128;
    unsigned rd_ = L_RED + 16 * lane;
    PIN(abg); PIN(abu); PIN(h0a); PIN(h1a); PIN(rd_);
    auto down_frags = [&]() {
#pragma unroll
      for (int t = 0; t < 4; ++t) {
        const unsigned ad = (A0 ^ (128 * (w & 1) + 32 * t)) + (DN0 + (w >> 1)) * HS;
        w8[2 * t] = cat(trfrag(L, ad), trfrag(L, ad + 2 * HS));
        w8[2 * t + 1] = cat(trfrag(L, ad + 4 * HS), v4u{wh[t][0], wh[t][1], 0u, 0u});
      }
    };
    bool early = false;
    {
      v4u u[4];
      const int off = T_P2 + 4 * w * 32768 + ((ph >> 3) & 1) * 16384 + own + (ph & 7) * 1024 + 16 * lane;
      POLL({ _Pragma("unroll") for (int k = 0; k < 4; ++k) u[k] = ld16(rs, off + k * 32768);
             FENCE();
             if (!early && (unsigned)__builtin_amdgcn_readfirstlane(lds_ld(L, L_FULL)) >= HEAD + NWP + 2 * NBLK + 6) { FENCE(); down_frags(); early = true; }
             FENCE();
             good = (((untag(u[0], t8) | untag(u[1], t8)) | (untag(u[2], t8) | untag(u[3], t8))) & 255u) == 0; });
      *(LAS f32x4*)(L + rd_ + 1024 * w) = (asf4(u[0]) + asf4(u[1])) + (asf4(u[2]) + asf4(u[3]));
    }
    LGKM0();
    __builtin_amdgcn_s_barrier();
    FENCE();
    {
      const unsigned o = rd_, oq = L_RED + 16 * 48;
      const f32x4 sq = (*(LAS f32x4*)(L + oq) + *(LAS f32x4*)(L + oq + 1024)) + (*(LAS f32x4*)(L + oq + 2048) + *(LAS f32x4*)(L + oq + 3072));
      const f32x4 pp = (*(LAS f32x4*)(L + o) + *(LAS f32x4*)(L + o + 1024)) + (*(LAS f32x4*)(L + o + 2048) + *(LAS f32x4*)(L + o + 3072));
      __builtin_amdgcn_sched_barrier(0);
      if (!early) down_frags();
      __builtin_amdgcn_sched_barrier(0);
      const float mean = sq[1] * (1.f / 1024.f), var = fmaxf(sq[3] * (1.f / 1024.f) - mean * mean, 0.f);
      const float mb = rbf(mean), id = rcp(rbf(__builtin_amdgcn_sqrtf(rbf(rbf(var) + 1e-5f))));
      const f32x2 nm = {-mb, -mb}, id2 = {id, id};
      const f32x2 g2 = __builtin_elementwise_fma(__builtin_elementwise_fma(nm, f32x2{abg[0], abg[1]}, f32x2{pp[0], pp[1]}), id2, f32x2{abg[2], abg[3]});
      const f32x2 u2 = __builtin_elementwise_fma(__builtin_elementwise_fma(nm, f32x2{abu[0], abu[1]}, f32x2{pp[2], pp[3]}), id2, f32x2{abu[2], abu[3]});
      const f32x2 gg = up2(pk2(g2[0], g2[1])), uu = up2(pk2(u2[0], u2[1]));
      const f32x2 in = (gg + gg * gg * gg * 0.044715f) * (2.f * 0.7978845608028654f * L2E);
      const f32x2 th = {1.f - 2.f * rcp(1.f + __builtin_amdgcn_exp2f(in[0])), 1.f - 2.f * rcp(1.f + __builtin_amdgcn_exp2f(in[1]))};
      const f32x2 ge = gg * 0.5f * (th + 1.f);
      const f32x2 hh = up2(pk2(ge[0], ge[1])) * uu;
      const unsigned hp = pk2(hh[0], g < 3 ? hh[1] : 0.f);
      *(LAS u16*)(L + h0a) = (u16)hp;
      *(LAS u16*)(L + h1a) = (u16)(hp >> 16);
    }
    LGKM0();
    gemv(so1 + (T_P3 - T_P1));
  } else {
    seen = 0;
    wait_ge(L, L_MRDY, 1, seen);
    unsigned ba[4];
#pragma unroll
    for (int j = 0; j < 4; ++j) ba[j] = (c >> 2) == j ? 128 * ((c >> 1) & 1) + 32 * g + 16 * (c & 1) : OOB;
    auto half_pass = [&](const v4u (&y)[4], int f0) {
      f32x4 a0 = Z, a1_ = Z;
#pragma unroll
      for (int j = 0; j < 4; ++j) { a0 = smfma(y[j], w8[f0 + j], a0, sidx); if (j < 3) a1_ = smfma(y[j], w8[f0 + 4 + j], a1_, sidx); }
      return f32x4{a0[0] + a0[1], a1_[0] + a1_[1], a0[2] + a0[3], a1_[2] + a1_[3]};
    };
    const int pc = lane & 3, col = 16 * w + 4 * pc + g;
    float ga = *(LAS float*)(L + L_MOD + 4 * col), c1 = rbf(1.f + *(LAS float*)(L + L_MOD + 512 + 4 * col));
    float xr = lo16(p.x[64 * a1 + col]);
    const int k4 = c >> 2;
    const unsigned yad = k4 < 2 ? L_Y + 128 * k4 + spos(col & ~1) + 2 * (g & 1) : L_X1 + 2 * col;
    {
      const f32x2 t = *(LAS f32x2*)(L + L_MOD + (lane < 32 ? 512 : 0) + 8 * lane);
      const float o = lane < 32 ? 1.f : 0.f;
      *(LAS unsigned*)(PV + 128 * (lane >> 5) + spos(2 * (lane & 31))) = pk2(o + t[0], o + t[1]);
    }
    LGKM0();
    const int a2 = 4 * b1 + w;
    int so2 = T_P2 + a1 * 32768 + ((a2 >> 3) & 1) * 16384 + ((b1 >> 2) ^ pi) * 8192 + (a2 & 7) * 1024 + 16 * lane;
    {
      v4u y[4];
#pragma unroll
      for (int j = 0; j < 4; ++j) y[j] = *(LAS v4u*)(L + (ba[j] == (unsigned)OOB ? L_ZERO : L_PRIV + 256 * w + ba[j]));
      const int off = T_AB + a1 * 65536 + (b1 & 3) * 16384 + ((b1 >> 2) ^ pi ^ 1) * 8192 + w * 2048 + 16 * lane;
      st16(rs, off, tg4(half_pass(y, 0), t8));
      unsigned ua[14];
#pragma unroll
      for (int f = 0; f < 14; ++f) { ua[f] = gu_addr(f >> 1, f & 1, UP0); PIN(ua[f]); }
      seen = 0;
      wait_ge(L, L_FULL, HEAD + NWQ + 2 * NBLK + (w < 2 ? 2 + 2 * w : 6), seen);
#pragma unroll
      for (int t = 0; t < 3; ++t) w8[7 + t] = cat(trfrag(L, ua[2 * t]), trfrag(L, ua[2 * t + 1]));
      wait_ge(L, L_FULL, HEAD + NWQ + 2 * NBLK + (w < 3 ? 2 + 2 * w : 7), seen);
#pragma unroll
      for (int t = 3; t < 7; ++t) w8[7 + t] = cat(trfrag(L, ua[2 * t]), trfrag(L, ua[2 * t + 1]));
      st16(rs, off + 1024, tg4(half_pass(y, 7), t8));
    }
    {
      v4u d[2];
      const int off = T_P1 + g * 16384 + own + (c >> 2) * 2048 + (a1 & 7) * 256 + 64 * w + 16 * pc;
      PIN(c1); PIN(xr); PIN(so2);
#pragma unroll
      for (int j = 0; j < 4; ++j) { ba[j] = ba[j] == (unsigned)OOB ? L_ZERO : L_Y + ba[j]; PIN(ba[j]); }
      POLL({ d[0] = ld16(rs, off); d[1] = ld16(rs, off + 65536); good = ((untag(d[0], t8) | untag(d[1], t8)) & 255u) == 0; });
      float at = fold4(asf4(d[0]) + asf4(d[1]));
      at += dpp<D_R8>(at);
      at += dpp<D_R4>(at);
      const unsigned x1 = pk2(xr + rbf(ga * rbf(at)), 0.f);
      const float y = lo16(x1) * c1;
      const unsigned hi = pk2(y, 0.f), lo = pk2(y - lo16(hi), 0.f);
      *(LAS u16*)(L + yad) = (u16)(k4 == 0 ? hi : k4 == 1 ? lo : x1);
    }
    LGKM0();
    __builtin_amdgcn_s_barrier();
    FENCE();
    {
      v4u y[4];
#pragma unroll
      for (int j = 0; j < 4; ++j) y[j] = *(LAS v4u*)(L + ba[j]);
      const v4u xx = *(LAS v4u*)(L + L_X1 + 16 * (lane & 7));
      __builtin_amdgcn_sched_barrier(0);
      const f32x4 G = half_pass(y, 0), U = half_pass(y, 7);
      float s = 0.f, q = 0.f;
#pragma unroll
      for (int e = 0; e < 4; ++e) { s = dot2(xx[e], 0x3F803F80u, s); q = dot2(xx[e], xx[e], q); }
      s += dpp<D_X1>(s); q += dpp<D_X1>(q);
      s += dpp<D_X2>(s); q += dpp<D_X2>(q);
      s += dpp<D_R4>(s); q += dpp<D_R4>(q);
      const f32x4 o = {G[0] + G[2], g < 3 ? G[1] + G[3] : s, U[0] + U[2], g < 3 ? U[1] + U[3] : q};
      st16(rs, so2, tg4(o, t8));
    }
    if (w == 0) {
      const int cl = 8 * b1 + 4 * (lane & 1);
      const float x1 = lo16(*(LAS u16*)(L + L_X1 + 2 * (cl + g))), gb = *(LAS float*)(L + L_MOD + 768 + 4 * (cl + g));
      __builtin_amdgcn_s_sleep(FSLEEP);
      v4u d[1];
      POLL({ d[0] = ld16(rs, T_P3 + (lane >> 3) * 16384 + own + ((lane >> 1) & 3) * 2048 + (a1 & 7) * 256 + 4 * cl); good = (untag(d[0], t8) & 255u) == 0; });
      float s = fold4(asf4(d[0]));
      s += dpp<D_R8>(s);
      s += dpp<D_R4>(s);
      s += dpp<D_X2>(s);
      if (c < 2) p.out[64 * a1 + cl + g] = (u16)pk2(x1 + rbf(gb * rbf(s)), 0.f);
    }
  }
}

extern "C" void kernel_launch(void* const* d_in, const int* in_sizes, int n_in, void* d_out, int out_size, void* d_ws, size_t ws_size,
                              hipStream_t stream) {
  if (n_in != 11 || out_size != 1024 || ws_size < (size_t)T_END + (2u << 20)) return;
  Params p;
  p.x = (const u16*)d_in[0]; p.K = (const u16*)d_in[1]; p.V = (const u16*)d_in[2]; p.wq = (const u16*)d_in[3]; p.wo = (const u16*)d_in[4];
  p.wg = (const u16*)d_in[6]; p.wu = (const u16*)d_in[7]; p.wd = (const u16*)d_in[8]; p.c = (const u16*)d_in[9]; p.wmod = (const u16*)d_in[10];
  p.out = (u16*)d_out; p.ws = (unsigned char*)d_ws;
  hipLaunchKernelGGL(fused_decode_block, dim3(256), dim3(384), 0, stream, p);
}
```
